```python
import torch

def _softmax(x, axis=-1):
    return torch.softmax(x, dim=axis)
def _gelu(x):
    return torch.nn.functional.gelu(x, approximate="tanh")
def _silu(x):
    return torch.nn.functional.silu(x)
def _sigmoid(x):
    return torch.sigmoid(x)
def _layernorm(x, axis=-1, eps=1e-5):
    m = x.mean(dim=axis, keepdim=True)
    v = x.var(dim=axis, keepdim=True, unbiased=False)
    return (x - m) / torch.sqrt(v + eps)
def _scatter_add(tgt, idx, src, axis=0):
    return tgt.clone().index_add_(axis, idx, src)
def _segment_sum(src, seg, n):
    out = torch.zeros((n,) + src.shape[1:], dtype=src.dtype)
    return out.index_add_(0, seg, src)
def _pad(x, pw):
    flat = tuple(v for pair in reversed(pw) for v in pair)
    return torch.nn.functional.pad(x, flat)

kernel_name = 'gated_mix__compressed_kv_cla__grouped_kv_mqa__prompt'

def setup_inputs():

    _w = (lambda a: (lambda t: t.to(torch.bfloat16) if t.is_floating_point() else t)(torch.as_tensor(a)))
    def _A_setup():
        B, H, Sq, Sk, D, M = 1, 32, 2048, 2048, 128, 512
        Dc = 4 * D
        return {
            "x": _w((lambda *s: torch.randn(*s, dtype=torch.bfloat16) * 0.05)(B, Sq, M)),
            "c_kv": _w((lambda *s: torch.randn(*s, dtype=torch.bfloat16) * 0.8)(B, Sk, Dc)),
            "w_q": _w((lambda *s: torch.randn(*s, dtype=torch.bfloat16) * 6.4)(M, H, D) * (M ** -0.5)),
            "w_uk": _w((lambda *s: torch.randn(*s, dtype=torch.bfloat16) * 6.4)(Dc, H, D) * (Dc ** -0.5)),
            "w_uv": _w((lambda *s: torch.randn(*s, dtype=torch.bfloat16) * 0.8)(Dc, H, D) * (Dc ** -0.5)),
            "w_o": _w((lambda *s: torch.randn(*s, dtype=torch.bfloat16) * 0.8)(H, D, M) * ((H * D) ** -0.5)),
        }
    _A_in = _A_setup()

    def _B_setup():
        B, S, D, H = 1, 2048, 512, 32
        Dh = D // H
        return {
            "x": _w((lambda *s: torch.randn(*s, dtype=torch.bfloat16) * 0.05)(B, S, D)),
            "wq": _w((lambda *s: torch.randn(*s, dtype=torch.bfloat16) * 3.2)(D, H * Dh)),
            "wk": _w((lambda *s: torch.randn(*s, dtype=torch.bfloat16) * 3.2)(D, Dh)),
            "wv": _w((lambda *s: torch.randn(*s, dtype=torch.bfloat16) * 0.1)(D, Dh)),
            "wo": _w((lambda *s: torch.randn(*s, dtype=torch.bfloat16) * 0.1)(H * Dh, D)),
        }
    _B_in = _B_setup()

    return {
        "A_x": _A_in["x"],
        "A_c_kv": _A_in["c_kv"],
        "A_w_q": _A_in["w_q"],
        "A_w_uk": _A_in["w_uk"],
        "A_w_uv": _A_in["w_uv"],
        "A_w_o": _A_in["w_o"],
        "B_x": _B_in["x"],
        "B_wq": _B_in["wq"],
        "B_wk": _B_in["wk"],
        "B_wv": _B_in["wv"],
        "B_wo": _B_in["wo"],
        "gate_w": _w((lambda *s: torch.randn(*s, dtype=torch.bfloat16) * 0.05)(512) * 16.0),
    }

def reference(A_x, A_c_kv, A_w_q, A_w_uk, A_w_uv, A_w_o, B_x, B_wq, B_wk, B_wv, B_wo, gate_w):
    def _A_ref(x, c_kv, w_q, w_uk, w_uv, w_o):
        B, Sq, M = x.shape[0], x.shape[1], x.shape[2]
        Sk = c_kv.shape[1]
        H, D = w_q.shape[1], w_q.shape[2]
        q = torch.einsum("bqm,mhd->bhqd", x, w_q)
        k = torch.einsum("bkc,chd->bhkd", c_kv, w_uk)
        v = torch.einsum("bkc,chd->bhkd", c_kv, w_uv)
        scores = torch.einsum("bhqd,bhkd->bhqk", q, k) * (D ** -0.5)
        off = Sk - Sq
        qi = torch.arange(Sq)[:, None]
        kj = torch.arange(Sk)[None, :]
        cmask = kj <= (qi + off)
        scores = torch.where(cmask[None, None], scores, -1e9)
        p = _softmax(scores, axis=-1)
        ctx = torch.einsum("bhqk,bhkd->bhqd", p, v)
        return torch.einsum("bhqd,hdm->bqm", ctx, w_o)

    def _B_ref(x, wq, wk, wv, wo):
        B, S, D = x.shape
        H = 32
        Dh = D // H
        q = (lambda x, *d: x.permute(*d))((x @ wq).reshape(B, S, H, Dh), 0, 2, 1, 3)
        k = x @ wk
        v = x @ wv
        scores = torch.einsum("bhqd,bkd->bhqk", q, k) * (Dh ** -0.5)
        p = _softmax(scores, axis=-1)
        o = torch.einsum("bhqk,bkd->bhqd", p, v)
        return (lambda x, *d: x.permute(*d))(o, 0, 2, 1, 3).reshape(B, S, H * Dh) @ wo

    x = A_x
    g = _sigmoid((x * gate_w).sum(-1, keepdims=True))
    a = _A_ref(x, A_c_kv, A_w_q, A_w_uk, A_w_uv, A_w_o)
    b = _B_ref(x, B_wq, B_wk, B_wv, B_wo)
    return g * a + (1 - g) * b
```

```cpp
#include <hip/hip_runtime.h>
#include <hip/hip_bf16.h>
#include <stdint.h>


typedef unsigned short bf16_t;
typedef short bf16x8 __attribute__((ext_vector_type(8)));
typedef short s16x4 __attribute__((ext_vector_type(4)));
typedef float f32x16 __attribute__((ext_vector_type(16)));
typedef float f32x8 __attribute__((ext_vector_type(8)));
typedef float f32x4 __attribute__((ext_vector_type(4)));
typedef unsigned u32x2 __attribute__((ext_vector_type(2)));
typedef unsigned u32x4 __attribute__((ext_vector_type(4)));
typedef __bf16 bf16v8 __attribute__((ext_vector_type(8)));
typedef __bf16 bf16v4 __attribute__((ext_vector_type(4)));
typedef __attribute__((address_space(3))) s16x4 lds_s16x4;

#define MFMA32(a, b, c) __builtin_amdgcn_mfma_f32_32x32x16_bf16((a), (b), (c), 0, 0, 0)
#define VMCNT(n) asm volatile("s_waitcnt vmcnt(%0)" ::"n"(n) : "memory")
#define BARRIER() __builtin_amdgcn_s_barrier()

__device__ __forceinline__ bf16x8 cvt8(f32x8 v) { return __builtin_bit_cast(bf16x8, __builtin_convertvector(v, bf16v8)); }
__device__ __forceinline__ u32x2 cvt4(f32x4 v) { return __builtin_bit_cast(u32x2, __builtin_convertvector(v, bf16v4)); }
__device__ __forceinline__ float bf2f(short v) { return __uint_as_float(((unsigned)(unsigned short)v) << 16); }
__device__ __forceinline__ float bfround(float x) { return (float)(__bf16)x; }
__device__ __forceinline__ int crow(int r, int hh) { return (r & 3) + 8 * (r >> 2) + 4 * hh; }
__device__ __forceinline__ void glds16(const void* g, char* lds) {
    __builtin_amdgcn_global_load_lds((const __attribute__((address_space(1))) void*)g,
                                     (__attribute__((address_space(3))) void*)lds, 16, 0, 0);
}
__device__ __forceinline__ s16x4 trread(const char* p) { return __builtin_amdgcn_ds_read_tr16_b64_v4i16((lds_s16x4*)p); }
__device__ __forceinline__ bf16x8 cat4(s16x4 lo, s16x4 hi) { return __builtin_shufflevector(lo, hi, 0, 1, 2, 3, 4, 5, 6, 7); }

struct ProjArgs {
    const bf16_t* x; const bf16_t* ckv; const bf16_t* wq; const bf16_t* wuk; const bf16_t* wuv;
    const bf16_t* bwq; const bf16_t* bwk; const bf16_t* bwv; const bf16_t* gw;
    bf16_t* Q; bf16_t* K; bf16_t* V; bf16_t* qB; bf16_t* kB; bf16_t* vB; float* g;
};

__global__ __launch_bounds__(512, 1) void proj_kernel(ProjArgs A) {
    __shared__ __attribute__((aligned(1024))) char smem[4 * 32768];
    const int tid = threadIdx.x, lane = tid & 63;
    const int w = __builtin_amdgcn_readfirstlane(tid >> 6);
    const int hh = lane >> 5, l31 = lane & 31;
    const int b = blockIdx.x;
    const int cg = (b & 7) * 4 + ((b >> 3) & 3), rb = b >> 5;
    const bool xtype = cg < 12;
    int t0, nt;
    if (xtype) { t0 = cg * 12; nt = (cg == 11) ? 13 : 12; }
    else { const int c = cg - 12; if (c < 16) { t0 = 13 * c; nt = 13; } else { t0 = 208 + 12 * (c - 16); nt = 12; } }
    const bf16_t* src = xtype ? A.x : A.ckv;
    const int row = rb * 256 + w * 32 + l31;

    auto issue = [&](int i) {
        const int tile = t0 + i;
        const bf16_t* W; int ldw, n0; bool special = false;
        if (xtype) {
            if (tile < 128) { W = A.wq; ldw = 4096; n0 = tile * 32; }
            else if (tile < 144) { W = A.bwq; ldw = 512; n0 = (tile - 128) * 32; }
            else { W = A.bwk; ldw = 16; n0 = 0; special = true; }
        } else {
            if (tile < 128) { W = A.wuk; ldw = 4096; n0 = tile * 32; }
            else { W = A.wuv; ldw = 4096; n0 = (tile - 128) * 32; }
        }
#pragma unroll
        for (int ii = 0; ii < 4; ++ii) {
            const int k = 16 * (4 * w + ii) + (lane >> 2), ch = lane & 3;
            const bf16_t* p = special ? ((ch < 2 ? A.bwk : A.bwv) + k * 16 + 8 * (ch & 1))
                                      : (W + (size_t)k * ldw + n0 + 8 * ch);
            glds16(p, smem + (i & 3) * 32768 + (4 * w + ii) * 1024);
        }
    };

    bf16x8 a[32];
#pragma unroll
    for (int s = 0; s < 32; ++s) a[s] = *(const bf16x8*)(src + (size_t)row * 512 + 16 * s + 8 * hh);
    issue(0); issue(1); issue(2);

    if (cg == 0) {
        float sum = 0.f;
#pragma unroll
        for (int s = 0; s < 32; ++s) {
            const bf16x8 gw = *(const bf16x8*)(A.gw + 16 * s + 8 * hh);
#pragma unroll
            for (int j = 0; j < 8; ++j) sum += bfround(bf2f(a[s][j]) * bf2f(gw[j]));
        }
        sum += __shfl_xor(sum, 32);
        if (hh == 0) {
            const float sb = bfround(sum);
            A.g[row] = bfround(1.0f / (1.0f + __expf(-sb)));
        }
    }

    const int g16 = lane >> 4, colhalf = g16 & 1, q4 = (lane & 15) >> 2, p4 = lane & 3;
    const int lane_off = (8 * hh + q4) * 64 + 32 * colhalf + 8 * p4;

    for (int i = 0; i < nt; ++i) {
        if (i == 0) VMCNT(8); else if (i == 1) VMCNT(12); else if (i == 2) VMCNT(16);
        else if (i + 2 < nt) VMCNT(20); else if (i + 1 < nt) VMCNT(16); else VMCNT(12);
        BARRIER();
        if (i + 3 < nt) issue(i + 3);
        const char* wb = smem + (i & 3) * 32768 + lane_off;
        f32x16 acc = {};
#pragma unroll
        for (int s = 0; s < 32; ++s) {
            const bf16x8 wf = cat4(trread(wb + s * 1024), trread(wb + s * 1024 + 256));
            acc = MFMA32(wf, a[s], acc);
        }
        const int tile = t0 + i;
        bf16_t* dst; bool special = false;
        if (xtype) {
            if (tile < 128) dst = A.Q + ((size_t)(tile >> 2) * 2048 + row) * 128 + (tile & 3) * 32;
            else if (tile < 144) dst = A.qB + (size_t)row * 512 + (tile - 128) * 32;
            else { dst = A.kB; special = true; }
        } else {
            bf16_t* base = tile < 128 ? A.K : A.V; const int t = tile & 127;
            dst = base + ((size_t)(t >> 2) * 2048 + row) * 128 + (t & 3) * 32;
        }
#pragma unroll
        for (int g4 = 0; g4 < 4; ++g4) {
            const u32x2 v = cvt4((f32x4){acc[4 * g4], acc[4 * g4 + 1], acc[4 * g4 + 2], acc[4 * g4 + 3]});
            bf16_t* d = special ? ((g4 < 2 ? A.kB : A.vB) + (size_t)row * 16 + 8 * (g4 & 1) + 4 * hh) : (dst + 8 * g4 + 4 * hh);
            *(u32x2*)d = v;
        }
    }
}

__global__ __launch_bounds__(512, 1) void attnA_kernel(const bf16_t* __restrict__ Qg, const bf16_t* __restrict__ Kg,
                                                       const bf16_t* __restrict__ Vg, bf16_t* __restrict__ ctx) {
    __shared__ __attribute__((aligned(1024))) char smem[131072];
    const int tid = threadIdx.x, lane = tid & 63;
    const int w = __builtin_amdgcn_readfirstlane(tid >> 6);
    const int rg = w & 3, kh = w >> 2, hh = lane >> 5, l31 = lane & 31;
    const int b = blockIdx.x, xq = 15 - (b >> 5), head = b & 31;
    const bf16_t* Qh = Qg + (size_t)head * 2048 * 128;
    const bf16_t* Kh = Kg + (size_t)head * 2048 * 128;
    const bf16_t* Vh = Vg + (size_t)head * 2048 * 128;
    const int qloc = rg * 32 + l31, qrow = xq * 128 + qloc;

    auto issue_tile = [&](int t, int buf) {
#pragma unroll
        for (int j = 0; j < 4; ++j) {
            const int ii = 4 * w + j, r = 4 * ii + (lane >> 4), c = lane & 15;
            glds16(Kh + (size_t)(t * 128 + r) * 128 + ((c ^ (r & 15)) * 8), smem + buf * 32768 + ii * 1024);
            glds16(Vh + (size_t)(t * 128 + r) * 128 + ((c ^ ((r & 3) << 2)) * 8), smem + 65536 + buf * 32768 + ii * 1024);
        }
    };
    issue_tile(0, 0);

    bf16x8 qf[8];
#pragma unroll
    for (int d0 = 0; d0 < 8; ++d0) qf[d0] = *(const bf16x8*)(Qh + (size_t)qrow * 128 + 16 * d0 + 8 * hh);

    int kaddr[8], vaddr[4];
    {
        const int key = 64 * kh + l31, base = key * 256 + ((hh ^ (key & 15)) << 4);
#pragma unroll
        for (int d0 = 0; d0 < 8; ++d0) kaddr[d0] = base ^ (d0 << 5);
        const int dhalf = (lane >> 4) & 1, q4 = (lane & 15) >> 2, p4 = lane & 3;
#pragma unroll
        for (int db = 0; db < 4; ++db) vaddr[db] = 65536 + (64 * kh + 4 * hh + q4) * 256 + ((db ^ q4) * 64) + 32 * dhalf + 8 * p4;
    }
    const float csc = 0.08838834764831845f * 1.4426950408889634f;
    f32x16 o[4] = {};
    float m = -1e30f, l = 0.f;

    VMCNT(0); BARRIER();
    for (int t = 0; t <= xq; ++t) {
        const int buf = t & 1;
        if (t < xq) issue_tile(t + 1, buf ^ 1);
        const bool diag = (t == xq);
        const bool skip = diag && kh == 1 && rg < 2;
        if (!skip) {
#pragma unroll
            for (int kb = 0; kb < 2; ++kb) {
                f32x16 s = {};
#pragma unroll
                for (int d0 = 0; d0 < 8; ++d0) {
                    const bf16x8 kf = *(const bf16x8*)(smem + buf * 32768 + kb * 8192 + kaddr[d0]);
                    s = MFMA32(kf, qf[d0], s);
                }
                if (diag) {
#pragma unroll
                    for (int r = 0; r < 16; ++r) if (64 * kh + 32 * kb + crow(r, hh) > qloc) s[r] = -__builtin_inff();
                }
                float smax = s[0];
#pragma unroll
                for (int r = 1; r < 16; ++r) smax = fmaxf(smax, s[r]);
                if (__any((smax - m) * csc > 8.0f)) {
                    const float rmax = fmaxf(smax, __shfl_xor(smax, 32));
                    const float mnew = fmaxf(m, rmax);
                    const float alpha = __builtin_amdgcn_exp2f((m - mnew) * csc);
                    l *= alpha;
#pragma unroll
                    for (int db = 0; db < 4; ++db)
#pragma unroll
                        for (int r = 0; r < 16; ++r) o[db][r] *= alpha;
                    m = mnew;
                }
                const float mc = m * csc;
                f32x8 p0, p1;
#pragma unroll
                for (int r = 0; r < 8; ++r) { p0[r] = __builtin_amdgcn_exp2f(s[r] * csc - mc); l += p0[r]; }
#pragma unroll
                for (int r = 0; r < 8; ++r) { p1[r] = __builtin_amdgcn_exp2f(s[8 + r] * csc - mc); l += p1[r]; }
                const bf16x8 pf0 = cvt8(p0), pf1 = cvt8(p1);
#pragma unroll
                for (int ss = 0; ss < 2; ++ss) {
                    const bf16x8 pf = ss ? pf1 : pf0;
#pragma unroll
                    for (int db = 0; db < 4; ++db) {
                        const char* vp = smem + buf * 32768 + vaddr[db] + (2 * kb + ss) * 4096;
                        const bf16x8 vf = cat4(trread(vp), trread(vp + 2048));
                        o[db] = MFMA32(vf, pf, o[db]);
                    }
                }
            }
        }
        VMCNT(0); BARRIER();
    }
    l += __shfl_xor(l, 32);
    float* mo = (float*)smem;
    float* st = (float*)(smem + 65536);
    if (kh == 1) {
#pragma unroll
        for (int db = 0; db < 4; ++db)
#pragma unroll
            for (int r = 0; r < 16; ++r) mo[(rg * 64 + db * 16 + r) * 64 + lane] = o[db][r];
        st[(rg * 2 + 0) * 64 + lane] = m;
        st[(rg * 2 + 1) * 64 + lane] = l;
    }
    __syncthreads();
    if (kh == 0) {
        const float m1 = st[(rg * 2 + 0) * 64 + lane], l1 = st[(rg * 2 + 1) * 64 + lane];
        const float mm = fmaxf(m, m1);
        const float a0 = __builtin_amdgcn_exp2f((m - mm) * csc), a1 = __builtin_amdgcn_exp2f((m1 - mm) * csc);
        const float inv = 1.0f / (l * a0 + l1 * a1);
        bf16_t* dst = ctx + (size_t)qrow * 4096 + head * 128 + 4 * hh;
#pragma unroll
        for (int db = 0; db < 4; ++db)
#pragma unroll
            for (int g4 = 0; g4 < 4; ++g4) {
                f32x4 v;
#pragma unroll
                for (int j = 0; j < 4; ++j) {
                    const int r = 4 * g4 + j;
                    v[j] = (o[db][r] * a0 + mo[(rg * 64 + db * 16 + r) * 64 + lane] * a1) * inv;
                }
                *(u32x2*)(dst + db * 32 + 8 * g4) = cvt4(v);
            }
    }
}

__global__ __launch_bounds__(512, 1) void attnB_kernel(const bf16_t* __restrict__ qB, const bf16_t* __restrict__ kB,
                                                       const bf16_t* __restrict__ vB, bf16_t* __restrict__ ctxB) {
    __shared__ __attribute__((aligned(1024))) char smem[131072 + 1024];
    const int tid = threadIdx.x, lane = tid & 63;
    const int w = __builtin_amdgcn_readfirstlane(tid >> 6);
    const int hh = lane >> 5, l31 = lane & 31;
#pragma unroll
    for (int j = 0; j < 8; ++j) {
        const int ci = (8 * w + j) * 64 + lane;
        glds16(kB + (size_t)(ci & 2047) * 16 + 8 * (ci >> 11), smem + (8 * w + j) * 1024);
        glds16(vB + (size_t)ci * 8, smem + 65536 + (8 * w + j) * 1024);
    }
    {
        const int c = tid & 15;
        ((bf16_t*)(smem + 131072))[tid] = (c == 0 || c == 4) ? (bf16_t)0x3F80 : (bf16_t)0;
    }
    const int qp = blockIdx.x * 8 + w;
    const bf16x8 qf = *(const bf16x8*)(qB + (size_t)qp * 512 + l31 * 16 + 8 * hh);
    VMCNT(0);
    __syncthreads();

    const int dhalf = (lane >> 4) & 1, q4 = (lane & 15) >> 2, p4 = lane & 3;
    const int vbase = dhalf ? (131072 + 8 * p4) : (65536 + (4 * hh + q4) * 32 + 8 * p4);
    const int vstep = dhalf ? 0 : 1024;
    const int kbase = (hh * 2048 + l31) * 16;
    const float csc = 0.25f * 1.4426950408889634f;
    f32x16 o = {};
    float m = -1e30f;
    for (int kt = 0; kt < 64; ++kt) {
        const bf16x8 kf = *(const bf16x8*)(smem + kbase + kt * 512);
        f32x16 s = {};
        s = MFMA32(kf, qf, s);
        float smax = s[0];
#pragma unroll
        for (int r = 1; r < 16; ++r) smax = fmaxf(smax, s[r]);
        if (__any((smax - m) * csc > 8.0f)) {
            const float rmax = fmaxf(smax, __shfl_xor(smax, 32));
            const float mnew = fmaxf(m, rmax);
            const float alpha = __builtin_amdgcn_exp2f((m - mnew) * csc);
#pragma unroll
            for (int r = 0; r < 16; ++r) o[r] *= alpha;
            m = mnew;
        }
        const float mc = m * csc;
        f32x8 p0, p1;
#pragma unroll
        for (int r = 0; r < 8; ++r) p0[r] = __builtin_amdgcn_exp2f(s[r] * csc - mc);
#pragma unroll
        for (int r = 0; r < 8; ++r) p1[r] = __builtin_amdgcn_exp2f(s[8 + r] * csc - mc);
        const bf16x8 pf0 = cvt8(p0), pf1 = cvt8(p1);
        const char* vp = smem + vbase + kt * vstep;
        const bf16x8 vf0 = cat4(trread(vp), trread(vp + 256));
        const bf16x8 vf1 = cat4(trread(vp + 512), trread(vp + 768));
        o = MFMA32(vf0, pf0, o);
        o = MFMA32(vf1, pf1, o);
    }
    const float inv = 1.0f / o[8];
    bf16_t* dst = ctxB + (size_t)qp * 512 + l31 * 16 + 4 * hh;
    *(u32x2*)(dst) = cvt4((f32x4){o[0] * inv, o[1] * inv, o[2] * inv, o[3] * inv});
    *(u32x2*)(dst + 8) = cvt4((f32x4){o[4] * inv, o[5] * inv, o[6] * inv, o[7] * inv});
}

__device__ __forceinline__ void gemm_job(const bf16_t* __restrict__ Ag, int lda, const bf16_t* __restrict__ Wg, int ldw,
                                         int k0, int klen, int m0, int n0, float* __restrict__ slab, char* smem,
                                         int lane, int w) {
    const int hh = lane >> 5, l31 = lane & 31;
    const int wm = w >> 1, wn = w & 1;
    const int nk = klen / 64;
    auto issue = [&](int kt, int buf) {
        const int kk = k0 + kt * 64;
#pragma unroll
        for (int j = 0; j < 4; ++j) {
            const int ii = 4 * w + j;
            {
                const int r = 8 * ii + (lane >> 3), c = lane & 7;
                glds16(Ag + (size_t)(m0 + r) * lda + kk + ((c ^ ((r >> 1) & 7)) * 8), smem + buf * 32768 + ii * 1024);
            }
            {
                const int sub = ii >> 2, k = 16 * (ii & 3) + (lane >> 2), ch = lane & 3;
                glds16(Wg + (size_t)(kk + k) * ldw + n0 + sub * 32 + 8 * ch, smem + buf * 32768 + 16384 + ii * 1024);
            }
        }
    };
    f32x16 acc[2][2] = {};
    const int g16 = lane >> 4, colhalf = g16 & 1, q4 = (lane & 15) >> 2, p4 = lane & 3;
    const int woff = (8 * hh + q4) * 64 + 32 * colhalf + 8 * p4;
    issue(0, 0);
    for (int kt = 0; kt < nk; ++kt) {
        const int buf = kt & 1;
        VMCNT(0); BARRIER();
        if (kt + 1 < nk) issue(kt + 1, buf ^ 1);
        const char* ab = smem + buf * 32768;
        const char* wb = smem + buf * 32768 + 16384 + woff;
#pragma unroll
        for (int s = 0; s < 4; ++s) {
            bf16x8 af[2], bfr[2];
#pragma unroll
            for (int mi = 0; mi < 2; ++mi) {
                const int r = wm * 64 + mi * 32 + l31;
                af[mi] = *(const bf16x8*)(ab + r * 128 + (((2 * s + hh) ^ ((r >> 1) & 7)) << 4));
            }
#pragma unroll
            for (int ni = 0; ni < 2; ++ni) {
                const char* p = wb + (wn * 2 + ni) * 4096 + s * 1024;
                bfr[ni] = cat4(trread(p), trread(p + 256));
            }
#pragma unroll
            for (int mi = 0; mi < 2; ++mi)
#pragma unroll
                for (int ni = 0; ni < 2; ++ni) acc[mi][ni] = MFMA32(af[mi], bfr[ni], acc[mi][ni]);
        }
    }
#pragma unroll
    for (int mi = 0; mi < 2; ++mi)
#pragma unroll
        for (int ni = 0; ni < 2; ++ni)
#pragma unroll
            for (int r = 0; r < 16; ++r) {
                const int mrow = m0 + wm * 64 + mi * 32 + crow(r, hh), ncol = n0 + wn * 64 + ni * 32 + l31;
                slab[(size_t)mrow * 512 + ncol] = acc[mi][ni][r];
            }
    __syncthreads();
}

__global__ __launch_bounds__(256, 2) void oproj_kernel(const bf16_t* __restrict__ ctxA, const bf16_t* __restrict__ wo,
                                                       const bf16_t* __restrict__ ctxB, const bf16_t* __restrict__ bwo,
                                                       float* __restrict__ slabs) {
    __shared__ __attribute__((aligned(1024))) char smem[65536];
    const int tid = threadIdx.x, lane = tid & 63;
    const int w = __builtin_amdgcn_readfirstlane(tid >> 6);
    const int b = blockIdx.x;
    const int z = b & 3, nt = (b >> 2) & 3, mt = b >> 4;
    const int m0 = mt * 128, n0 = nt * 128;
    const size_t SL = (size_t)2048 * 512;
    if (z < 3) {
        gemm_job(ctxA, 4096, wo, 512, 1152 * z, 1152, m0, n0, slabs + z * SL, smem, lane, w);
    } else {
        gemm_job(ctxA, 4096, wo, 512, 3456, 640, m0, n0, slabs + 3 * SL, smem, lane, w);
        gemm_job(ctxB, 512, bwo, 512, 0, 512, m0, n0, slabs + 4 * SL, smem, lane, w);
    }
}

__global__ __launch_bounds__(256) void reduce_kernel(const float* __restrict__ slabs, const float* __restrict__ g,
                                                     bf16_t* __restrict__ out) {
    const size_t SL = (size_t)2048 * 512;
    const int idx = (blockIdx.x * 256 + threadIdx.x) * 4;
    const int row = idx >> 9;
    f32x4 a = *(const f32x4*)(slabs + idx);
#pragma unroll
    for (int z = 1; z < 4; ++z) a += *(const f32x4*)(slabs + z * SL + idx);
    const f32x4 bb = *(const f32x4*)(slabs + 4 * SL + idx);
    const float gg = g[row], og = bfround(1.0f - gg);
    f32x4 r;
#pragma unroll
    for (int j = 0; j < 4; ++j) r[j] = bfround(gg * bfround(a[j])) + bfround(og * bfround(bb[j]));
    *(u32x2*)(out + idx) = cvt4(r);
}

extern "C" void kernel_launch(void* const* d_in, const int* in_sizes, int n_in, void* d_out, int out_size, void* d_ws,
                              size_t ws_size, hipStream_t stream) {
    (void)in_sizes; (void)n_in; (void)out_size; (void)ws_size;
    char* ws = (char*)d_ws;
    const size_t MB = 1u << 20;
    ProjArgs A;
    A.x = (const bf16_t*)d_in[0]; A.ckv = (const bf16_t*)d_in[1]; A.wq = (const bf16_t*)d_in[2];
    A.wuk = (const bf16_t*)d_in[3]; A.wuv = (const bf16_t*)d_in[4];
    const bf16_t* wo = (const bf16_t*)d_in[5];
    A.bwq = (const bf16_t*)d_in[7]; A.bwk = (const bf16_t*)d_in[8]; A.bwv = (const bf16_t*)d_in[9];
    const bf16_t* bwo = (const bf16_t*)d_in[10];
    A.gw = (const bf16_t*)d_in[11];
    A.Q = (bf16_t*)(ws + 0 * MB); A.K = (bf16_t*)(ws + 16 * MB); A.V = (bf16_t*)(ws + 32 * MB);
    A.qB = (bf16_t*)(ws + 48 * MB); A.kB = (bf16_t*)(ws + 50 * MB); A.vB = (bf16_t*)(ws + 51 * MB);
    A.g = (float*)(ws + 52 * MB);
    bf16_t* ctxA = (bf16_t*)(ws + 56 * MB);
    bf16_t* ctxB = (bf16_t*)(ws + 72 * MB);
    float* slabs = (float*)(ws + 80 * MB);

    proj_kernel<<<256, 512, 0, stream>>>(A);
    attnA_kernel<<<512, 512, 0, stream>>>(A.Q, A.K, A.V, ctxA);
    attnB_kernel<<<256, 512, 0, stream>>>(A.qB, A.kB, A.vB, ctxB);
    oproj_kernel<<<256, 256, 0, stream>>>(ctxA, wo, ctxB, bwo, slabs);
    reduce_kernel<<<1024, 256, 0, stream>>>(slabs, A.g, (bf16_t*)d_out);
}
```

```cpp
#include <hip/hip_runtime.h>
#include <hip/hip_bf16.h>
#include <stdint.h>

typedef unsigned short bf16_t;
typedef short bf16x8 __attribute__((ext_vector_type(8)));
typedef short s16x4 __attribute__((ext_vector_type(4)));
typedef float f32x16 __attribute__((ext_vector_type(16)));
typedef float f32x8 __attribute__((ext_vector_type(8)));
typedef float f32x4 __attribute__((ext_vector_type(4)));
typedef float f32x2 __attribute__((ext_vector_type(2)));
typedef unsigned u32x2 __attribute__((ext_vector_type(2)));
typedef unsigned u32x4 __attribute__((ext_vector_type(4)));
typedef __bf16 bf16v8 __attribute__((ext_vector_type(8)));
typedef __bf16 bf16v4 __attribute__((ext_vector_type(4)));
typedef __attribute__((address_space(3))) s16x4 lds_s16x4;

#define USE_SGB 1
#define MFMA32(a, b, c) __builtin_amdgcn_mfma_f32_32x32x16_bf16((a), (b), (c), 0, 0, 0)
#define VMCNT(n) asm volatile("s_waitcnt vmcnt(%0)" ::"n"(n) : "memory")
#define BARRIER() __builtin_amdgcn_s_barrier()

__device__ __forceinline__ bf16x8 cvt8(f32x8 v) { return __builtin_bit_cast(bf16x8, __builtin_convertvector(v, bf16v8)); }
__device__ __forceinline__ u32x2 cvt4(f32x4 v) { return __builtin_bit_cast(u32x2, __builtin_convertvector(v, bf16v4)); }
__device__ __forceinline__ float bf2f(short v) { return __uint_as_float(((unsigned)(unsigned short)v) << 16); }
__device__ __forceinline__ float bfround(float x) { return (float)(__bf16)x; }
__device__ __forceinline__ int crow(int r, int hh) { return (r & 3) + 8 * (r >> 2) + 4 * hh; }
__device__ __forceinline__ void glds16(const void* g, char* lds) {
    const unsigned a = __builtin_amdgcn_readfirstlane((unsigned)(size_t)((__attribute__((address_space(3))) char*)lds));
    unsigned keep;
    asm volatile("s_mov_b32 %0, m0\n\ts_mov_b32 m0, %2\n\ts_nop 0\n\tglobal_load_lds_dwordx4 %1, off\n\ts_mov_b32 m0, %0"
                 : "=&s"(keep) : "v"(g), "s"(a) : "memory");
}
__device__ __forceinline__ s16x4 trread(const char* p) { return __builtin_amdgcn_ds_read_tr16_b64_v4i16((lds_s16x4*)p); }
__device__ __forceinline__ bf16x8 cat4(s16x4 lo, s16x4 hi) { return __builtin_shufflevector(lo, hi, 0, 1, 2, 3, 4, 5, 6, 7); }

struct ProjArgs {
    const bf16_t* x; const bf16_t* ckv; const bf16_t* wq; const bf16_t* wuk; const bf16_t* wuv;
    const bf16_t* bwq; const bf16_t* bwk; const bf16_t* bwv; const bf16_t* gw;
    bf16_t* Q; bf16_t* K; bf16_t* V; bf16_t* qB; bf16_t* kB; bf16_t* vB; float* g;
};

__global__ __launch_bounds__(512, 1) void proj_kernel(ProjArgs A) {
    __shared__ __attribute__((aligned(1024))) char smem[4 * 32768 + 1024];
    const int tid = threadIdx.x, lane = tid & 63;
    const int w = __builtin_amdgcn_readfirstlane(tid >> 6);
    const int hh = lane >> 5, l31 = lane & 31;
    const int b = blockIdx.x;
    const int cg = (b & 7) * 4 + ((b >> 3) & 3), rb = b >> 5;
    const bool xtype = cg < 12;
    int t0, nt;
    if (xtype) { t0 = cg * 12; nt = (cg == 11) ? 13 : 12; }
    else { const int c = cg - 12; if (c < 16) { t0 = 13 * c; nt = 13; } else { t0 = 208 + 12 * (c - 16); nt = 12; } }
    const bf16_t* src = xtype ? A.x : A.ckv;
    const int row = rb * 256 + w * 32 + l31;

    auto issue = [&](int i) {
        const int tile = t0 + i;
        const bf16_t* W; int ldw, n0; bool special = false;
        if (xtype) {
            if (tile < 128) { W = A.wq; ldw = 4096; n0 = tile * 32; }
            else if (tile < 144) { W = A.bwq; ldw = 512; n0 = (tile - 128) * 32; }
            else { W = A.bwk; ldw = 16; n0 = 0; special = true; }
        } else {
            if (tile < 128) { W = A.wuk; ldw = 4096; n0 = tile * 32; }
            else { W = A.wuv; ldw = 4096; n0 = (tile - 128) * 32; }
        }
#pragma unroll
        for (int ii = 0; ii < 4; ++ii) {
            const int k = 16 * (4 * w + ii) + (lane >> 2), ch = lane & 3;
            const bf16_t* p = special ? ((ch < 2 ? A.bwk : A.bwv) + k * 16 + 8 * (ch & 1))
                                      : (W + (size_t)k * ldw + n0 + 8 * ch);
            glds16(p, smem + (i & 3) * 32768 + (4 * w + ii) * 1024);
        }
    };

    bf16x8 a[32];
    if (w == 0) glds16(A.gw + lane * 8, smem + 131072);
#pragma unroll
#define ABL_ROUNDS 2
    for (int r = 0; r < ABL_ROUNDS; ++r) {
#pragma unroll
        for (int j = 0; j < 16; ++j) {
            const int rl = 16 * w + j;
            glds16(src + (size_t)(rb * 256 + r * 128 + rl) * 512 + ((lane ^ (rl & 15)) * 8), smem + rl * 1024);
        }
        VMCNT(0); BARRIER();
        if ((w >> 2) == r) {
            const int rl = (w & 3) * 32 + l31;
#pragma unroll
            for (int s = 0; s < 32; ++s) a[s] = *(const bf16x8*)(smem + rl * 1024 + (((2 * s + hh) ^ (rl & 15)) << 4));
        }
        asm volatile("s_waitcnt lgkmcnt(0)" ::: "memory");
        BARRIER();
    }
    issue(0); issue(1); issue(2);

    if (cg == 0) {
        float sum = 0.f;
#pragma unroll
        for (int s = 0; s < 32; ++s) {
            const bf16x8 gw = *(const bf16x8*)(smem + 131072 + (2 * s + hh) * 16);
#pragma unroll
            for (int j = 0; j < 8; ++j) sum += bfround(bf2f(a[s][j]) * bf2f(gw[j]));
        }
        sum += __shfl_xor(sum, 32);
        if (hh == 0) {
            const float sb = bfround(sum);
            A.g[row] = bfround(1.0f / (1.0f + __expf(-sb)));
        }
    }

    const int g16 = lane >> 4, colhalf = g16 & 1, q4 = (lane & 15) >> 2, p4 = lane & 3;
    const int lane_off = (8 * hh + q4) * 64 + 32 * colhalf + 8 * p4;

    for (int i = 0; i < nt; ++i) {
        if (i == 0) VMCNT(8); else if (i == 1) VMCNT(12); else if (i == 2) VMCNT(16);
        else if (i + 2 < nt) VMCNT(20); else if (i + 1 < nt) VMCNT(16); else VMCNT(12);
        BARRIER();
        if (i + 3 < nt) issue(i + 3);
        const char* wb = smem + (i & 3) * 32768 + lane_off;
        f32x16 acc = {};
        constexpr int LA = 4;
        bf16x8 wf[LA];
#pragma unroll
        for (int s = 0; s < LA; ++s) wf[s] = cat4(trread(wb + s * 1024), trread(wb + s * 1024 + 256));
        __builtin_amdgcn_sched_group_barrier(0x100, 2 * LA, 0);
#pragma unroll
        for (int s = 0; s < 32; ++s) {
            acc = MFMA32(wf[s % LA], a[s], acc);
            if (s + LA < 32) wf[s % LA] = cat4(trread(wb + (s + LA) * 1024), trread(wb + (s + LA) * 1024 + 256));
            __builtin_amdgcn_sched_group_barrier(0x8, 1, 0);
            __builtin_amdgcn_sched_group_barrier(0x100, 2, 0);
        }
        const int tile = t0 + i;
        bf16_t* dst; bool special = false;
        if (xtype) {
            if (tile < 128) dst = A.Q + ((size_t)(tile >> 2) * 2048 + row) * 128 + (tile & 3) * 32;
            else if (tile < 144) dst = A.qB + (size_t)row * 512 + (tile - 128) * 32;
            else { dst = A.kB; special = true; }
        } else {
            bf16_t* base = tile < 128 ? A.K : A.V; const int t = tile & 127;
            dst = base + ((size_t)(t >> 2) * 2048 + row) * 128 + (t & 3) * 32;
        }
#pragma unroll
        for (int g4 = 0; g4 < 4; ++g4) {
            const u32x2 v = cvt4((f32x4){acc[4 * g4], acc[4 * g4 + 1], acc[4 * g4 + 2], acc[4 * g4 + 3]});
            bf16_t* d = special ? ((g4 < 2 ? A.kB : A.vB) + (size_t)row * 16 + 8 * (g4 & 1) + 4 * hh) : (dst + 8 * g4 + 4 * hh);
            *(u32x2*)d = v;
        }
    }
}

__global__ __launch_bounds__(512, 1) void attnA_kernel(const bf16_t* __restrict__ Qg, const bf16_t* __restrict__ Kg,
                                                       const bf16_t* __restrict__ Vg, bf16_t* __restrict__ ctx) {
    __shared__ __attribute__((aligned(1024))) char smem[163840];
    const int tid = threadIdx.x, lane = tid & 63;
    const int w = __builtin_amdgcn_readfirstlane(tid >> 6);
    const int rg = w & 3, kh = w >> 2, hh = lane >> 5, l31 = lane & 31;
    const int b = blockIdx.x, xq = 15 - (b >> 5), head = b & 31;
    const bf16_t* Qh = Qg + (size_t)head * 2048 * 128;
    const bf16_t* Kh = Kg + (size_t)head * 2048 * 128;
    const bf16_t* Vh = Vg + (size_t)head * 2048 * 128;
    const int qloc = rg * 32 + l31, qrow = xq * 128 + qloc;

    auto issue_k = [&](int t) {
        const int kb = t % 3;
#pragma unroll
        for (int j = 0; j < 4; ++j) {
            const int ii = 4 * w + j, r = 4 * ii + (lane >> 4), c = lane & 15;
            glds16(Kh + (size_t)(t * 128 + r) * 128 + ((c ^ (r & 15)) * 8), smem + kb * 32768 + ii * 1024);
        }
    };
    auto issue_v = [&](int t) {
#pragma unroll
        for (int j = 0; j < 4; ++j) {
            const int ii = 4 * w + j, r = 4 * ii + (lane >> 4), c = lane & 15;
            glds16(Vh + (size_t)(t * 128 + r) * 128 + ((c ^ ((r & 3) << 2)) * 8), smem + 98304 + (t & 1) * 32768 + ii * 1024);
        }
    };
    issue_k(0); issue_v(0); if (xq > 0) issue_k(1);

    bf16x8 qf[8];
#pragma unroll
    for (int d0 = 0; d0 < 8; ++d0) qf[d0] = *(const bf16x8*)(Qh + (size_t)qrow * 128 + 16 * d0 + 8 * hh);

    int kaddr[8], vaddr[4];
    {
        const int key = 64 * kh + l31, base = key * 256 + ((hh ^ (key & 15)) << 4);
#pragma unroll
        for (int d0 = 0; d0 < 8; ++d0) kaddr[d0] = base ^ (d0 << 5);
        const int dhalf = (lane >> 4) & 1, q4 = (lane & 15) >> 2, p4 = lane & 3;
#pragma unroll
        for (int db = 0; db < 4; ++db) vaddr[db] = 98304 + (64 * kh + 4 * hh + q4) * 256 + ((db ^ q4) * 64) + 32 * dhalf + 8 * p4;
    }
    const float csc = 0.08838834764831845f * 1.4426950408889634f;
    f32x16 o[4] = {};
    float m = -1e30f, l = 0.f;
    f32x16 s0, s1;

    auto qk = [&](int t, f32x16& a0, f32x16& a1) {
        const char* kb = smem + (t % 3) * 32768;
        a0 = f32x16{}; a1 = f32x16{};
#pragma unroll
        for (int d0 = 0; d0 < 8; ++d0) {
            const bf16x8 k0 = *(const bf16x8*)(kb + kaddr[d0]);
            const bf16x8 k1 = *(const bf16x8*)(kb + 8192 + kaddr[d0]);
            a0 = MFMA32(k0, qf[d0], a0);
            a1 = MFMA32(k1, qf[d0], a1);
        }
        if (t == xq) {
#pragma unroll
            for (int r = 0; r < 16; ++r) {
                if (64 * kh + crow(r, hh) > qloc) a0[r] = -__builtin_inff();
                if (64 * kh + 32 + crow(r, hh) > qloc) a1[r] = -__builtin_inff();
            }
        }
    };

    VMCNT(0); BARRIER();
    qk(0, s0, s1);
    for (int t = 0; t <= xq; ++t) {
        if (t + 2 <= xq) issue_k(t + 2);
        if (t + 1 <= xq) issue_v(t + 1);
        float smax = fmaxf(s0[0], s1[0]);
#pragma unroll
        for (int r = 1; r < 16; ++r) smax = fmaxf(smax, fmaxf(s0[r], s1[r]));
        if (__any((smax - m) * csc > 8.0f)) {
            const float rmax = fmaxf(smax, __shfl_xor(smax, 32));
            const float mnew = fmaxf(m, rmax);
            const float alpha = __builtin_amdgcn_exp2f((m - mnew) * csc);
            l *= alpha;
#pragma unroll
            for (int db = 0; db < 4; ++db)
#pragma unroll
                for (int r = 0; r < 16; ++r) o[db][r] *= alpha;
            m = mnew;
        }
        const float mc = m * csc;
        bf16x8 pf[4];
#pragma unroll
        for (int hb = 0; hb < 4; ++hb) {
            f32x8 p;
#pragma unroll
            for (int r = 0; r < 8; ++r) {
                const float sv = (hb < 2) ? s0[8 * hb + r] : s1[8 * (hb - 2) + r];
                p[r] = __builtin_amdgcn_exp2f(sv * csc - mc); l += p[r];
            }
            pf[hb] = cvt8(p);
        }
        f32x16 n0, n1;
        if (t < xq) qk(t + 1, n0, n1);
        const char* vb = smem + (t & 1) * 32768;
#pragma unroll
        for (int ks = 0; ks < 4; ++ks) {
#pragma unroll
            for (int db = 0; db < 4; ++db) {
                const char* vp = vb + vaddr[db] + ks * 4096;
                const bf16x8 vf = cat4(trread(vp), trread(vp + 2048));
                o[db] = MFMA32(vf, pf[ks], o[db]);
            }
        }
        if (t < xq) { s0 = n0; s1 = n1; }
        VMCNT(0); BARRIER();
    }
    l += __shfl_xor(l, 32);
    float* mo = (float*)smem;
    float* st = (float*)(smem + 65536);
    auto give = [&](const f32x16& a0, const f32x16& a1, int db0) {
#pragma unroll
        for (int r = 0; r < 16; ++r) {
            mo[(rg * 64 + db0 * 16 + r) * 64 + lane] = a0[r];
            mo[(rg * 64 + (db0 + 1) * 16 + r) * 64 + lane] = a1[r];
        }
    };
    if (kh == 0) give(o[2], o[3], 2); else give(o[0], o[1], 0);
    st[((rg * 2 + kh) * 2 + 0) * 64 + lane] = m;
    st[((rg * 2 + kh) * 2 + 1) * 64 + lane] = l;
    __syncthreads();
    {
        const float m1 = st[((rg * 2 + (kh ^ 1)) * 2 + 0) * 64 + lane], l1 = st[((rg * 2 + (kh ^ 1)) * 2 + 1) * 64 + lane];
        const float mm = fmaxf(m, m1);
        const float a0 = __builtin_amdgcn_exp2f((m - mm) * csc), a1 = __builtin_amdgcn_exp2f((m1 - mm) * csc);
        const float inv = 1.0f / (l * a0 + l1 * a1);
        bf16_t* dst = ctx + (size_t)qrow * 4096 + head * 128 + 4 * hh;
        auto fin = [&](const f32x16& oo, int db) {
#pragma unroll
            for (int g4 = 0; g4 < 4; ++g4) {
                f32x4 v;
#pragma unroll
                for (int j = 0; j < 4; ++j) {
                    const int r = 4 * g4 + j;
                    v[j] = (oo[r] * a0 + mo[(rg * 64 + db * 16 + r) * 64 + lane] * a1) * inv;
                }
                *(u32x2*)(dst + db * 32 + 8 * g4) = cvt4(v);
            }
        };
        if (kh == 0) { fin(o[0], 0); fin(o[1], 1); } else { fin(o[2], 2); fin(o[3], 3); }
    }
}

__global__ __launch_bounds__(512, 1) void attnB_kernel(const bf16_t* __restrict__ qB, const bf16_t* __restrict__ kB,
                                                       const bf16_t* __restrict__ vB, bf16_t* __restrict__ ctxB) {
    __shared__ __attribute__((aligned(1024))) char smem[131072 + 1024];
    const int tid = threadIdx.x, lane = tid & 63;
    const int w = __builtin_amdgcn_readfirstlane(tid >> 6);
    const int hh = lane >> 5, l31 = lane & 31;
#pragma unroll
    for (int j = 0; j < 8; ++j) {
        const int ci = (8 * w + j) * 64 + lane;
        glds16(kB + (size_t)(ci & 2047) * 16 + 8 * (ci >> 11), smem + (8 * w + j) * 1024);
        glds16(vB + (size_t)ci * 8, smem + 65536 + (8 * w + j) * 1024);
    }
    {
        const int c = tid & 15;
        ((bf16_t*)(smem + 131072))[tid] = (c == 0 || c == 4) ? (bf16_t)0x3F80 : (bf16_t)0;
    }
    const int qp = blockIdx.x * 8 + w;
    const bf16x8 qf = *(const bf16x8*)(qB + (size_t)qp * 512 + l31 * 16 + 8 * hh);
    VMCNT(0);
    __syncthreads();

    const int dhalf = (lane >> 4) & 1, q4 = (lane & 15) >> 2, p4 = lane & 3;
    const int vbase = dhalf ? (131072 + 8 * p4) : (65536 + (4 * hh + q4) * 32 + 8 * p4);
    const int vstep = dhalf ? 0 : 1024;
    const int kbase = (hh * 2048 + l31) * 16;
    const float csc = 0.25f * 1.4426950408889634f;
    f32x16 o = {};
    float m = -1e30f;
    for (int kt = 0; kt < 64; kt += 4) {
        f32x16 s[4];
#pragma unroll
        for (int i = 0; i < 4; ++i) {
            const bf16x8 kf = *(const bf16x8*)(smem + kbase + (kt + i) * 512);
            f32x16 z = {};
            s[i] = MFMA32(kf, qf, z);
        }
        float smax = s[0][0];
#pragma unroll
        for (int i = 0; i < 4; ++i)
#pragma unroll
            for (int r = 0; r < 16; ++r) smax = fmaxf(smax, s[i][r]);
        if (__any((smax - m) * csc > 8.0f)) {
            const float rmax = fmaxf(smax, __shfl_xor(smax, 32));
            const float mnew = fmaxf(m, rmax);
            const float alpha = __builtin_amdgcn_exp2f((m - mnew) * csc);
#pragma unroll
            for (int r = 0; r < 16; ++r) o[r] *= alpha;
            m = mnew;
        }
        const float mc = m * csc;
#pragma unroll
        for (int i = 0; i < 4; ++i) {
            f32x8 p0, p1;
#pragma unroll
            for (int r = 0; r < 8; r += 2) {
                const f32x2 t = __builtin_elementwise_fma((f32x2){s[i][r], s[i][r + 1]}, (f32x2){csc, csc}, (f32x2){-mc, -mc});
                p0[r] = __builtin_amdgcn_exp2f(t[0]); p0[r + 1] = __builtin_amdgcn_exp2f(t[1]);
            }
#pragma unroll
            for (int r = 0; r < 8; r += 2) {
                const f32x2 t = __builtin_elementwise_fma((f32x2){s[i][8 + r], s[i][9 + r]}, (f32x2){csc, csc}, (f32x2){-mc, -mc});
                p1[r] = __builtin_amdgcn_exp2f(t[0]); p1[r + 1] = __builtin_amdgcn_exp2f(t[1]);
            }
            const bf16x8 pf0 = cvt8(p0), pf1 = cvt8(p1);
            const char* vp = smem + vbase + (kt + i) * vstep;
            const bf16x8 vf0 = cat4(trread(vp), trread(vp + 256));
            const bf16x8 vf1 = cat4(trread(vp + 512), trread(vp + 768));
            o = MFMA32(vf0, pf0, o);
            o = MFMA32(vf1, pf1, o);
        }
    }
    const float inv = 1.0f / o[8];
    bf16_t* dst = ctxB + (size_t)qp * 512 + l31 * 16 + 4 * hh;
    *(u32x2*)(dst) = cvt4((f32x4){o[0] * inv, o[1] * inv, o[2] * inv, o[3] * inv});
    *(u32x2*)(dst + 8) = cvt4((f32x4){o[4] * inv, o[5] * inv, o[6] * inv, o[7] * inv});
}

__device__ __forceinline__ void gemm_job(const bf16_t* __restrict__ Ag, int lda, const bf16_t* __restrict__ Wg, int ldw,
                                         int k0, int klen, int m0, int n0, float* __restrict__ slab, char* smem,
                                         int lane, int w) {
    const int hh = lane >> 5, l31 = lane & 31;
    const int wm = w >> 1, wn = w & 1;
    const int nk = klen / 64;
    auto issue = [&](int kt, int buf) {
        const int kk = k0 + kt * 64;
#pragma unroll
        for (int j = 0; j < 4; ++j) {
            const int ii = 4 * w + j;
            {
                const int r = 8 * ii + (lane >> 3), c = lane & 7;
                glds16(Ag + (size_t)(m0 + r) * lda + kk + ((c ^ ((r >> 1) & 7)) * 8), smem + buf * 32768 + ii * 1024);
            }
            {
                const int sub = ii >> 2, k = 16 * (ii & 3) + (lane >> 2), ch = lane & 3;
                glds16(Wg + (size_t)(kk + k) * ldw + n0 + sub * 32 + 8 * ch, smem + buf * 32768 + 16384 + ii * 1024);
            }
        }
    };
    f32x16 acc[2][2] = {};
    const int g16 = lane >> 4, colhalf = g16 & 1, q4 = (lane & 15) >> 2, p4 = lane & 3;
    const int woff = (8 * hh + q4) * 64 + 32 * colhalf + 8 * p4;
    issue(0, 0);
    for (int kt = 0; kt < nk; ++kt) {
        const int buf = kt & 1;
        VMCNT(0); BARRIER();
        if (kt + 1 < nk) issue(kt + 1, buf ^ 1);
        const char* ab = smem + buf * 32768;
        const char* wb = smem + buf * 32768 + 16384 + woff;
#pragma unroll
        for (int s = 0; s < 4; ++s) {
            bf16x8 af[2], bfr[2];
#pragma unroll
            for (int mi = 0; mi < 2; ++mi) {
                const int r = wm * 64 + mi * 32 + l31;
                af[mi] = *(const bf16x8*)(ab + r * 128 + (((2 * s + hh) ^ ((r >> 1) & 7)) << 4));
            }
#pragma unroll
            for (int ni = 0; ni < 2; ++ni) {
                const char* p = wb + (wn * 2 + ni) * 4096 + s * 1024;
                bfr[ni] = cat4(trread(p), trread(p + 256));
            }
#pragma unroll
            for (int mi = 0; mi < 2; ++mi)
#pragma unroll
                for (int ni = 0; ni < 2; ++ni) acc[mi][ni] = MFMA32(af[mi], bfr[ni], acc[mi][ni]);
        }
    }
#pragma unroll
    for (int mi = 0; mi < 2; ++mi)
#pragma unroll
        for (int ni = 0; ni < 2; ++ni)
#pragma unroll
            for (int r = 0; r < 16; ++r) {
                const int mrow = m0 + wm * 64 + mi * 32 + crow(r, hh), ncol = n0 + wn * 64 + ni * 32 + l31;
                slab[(size_t)mrow * 512 + ncol] = acc[mi][ni][r];
            }
    __syncthreads();
}

__global__ __launch_bounds__(256, 2) void oproj_kernel(const bf16_t* __restrict__ ctxA, const bf16_t* __restrict__ wo,
                                                       const bf16_t* __restrict__ ctxB, const bf16_t* __restrict__ bwo,
                                                       float* __restrict__ slabs) {
    __shared__ __attribute__((aligned(1024))) char smem[65536];
    const int tid = threadIdx.x, lane = tid & 63;
    const int w = __builtin_amdgcn_readfirstlane(tid >> 6);
    const int b = blockIdx.x;
    const int z = b & 3, nt = (b >> 2) & 3, mt = b >> 4;
    const int m0 = mt * 128, n0 = nt * 128;
    const size_t SL = (size_t)2048 * 512;
    if (z < 3) {
        gemm_job(ctxA, 4096, wo, 512, 1152 * z, 1152, m0, n0, slabs + z * SL, smem, lane, w);
    } else {
        gemm_job(ctxA, 4096, wo, 512, 3456, 640, m0, n0, slabs + 3 * SL, smem, lane, w);
        gemm_job(ctxB, 512, bwo, 512, 0, 512, m0, n0, slabs + 4 * SL, smem, lane, w);
    }
}

__global__ __launch_bounds__(256) void reduce_kernel(const float* __restrict__ slabs, const float* __restrict__ g,
                                                     bf16_t* __restrict__ out) {
    const size_t SL = (size_t)2048 * 512;
    const int idx = (blockIdx.x * 256 + threadIdx.x) * 4;
    const int row = idx >> 9;
    f32x4 a = *(const f32x4*)(slabs + idx);
#pragma unroll
    for (int z = 1; z < 4; ++z) a += *(const f32x4*)(slabs + z * SL + idx);
    const f32x4 bb = *(const f32x4*)(slabs + 4 * SL + idx);
    const float gg = g[row], og = bfround(1.0f - gg);
    f32x4 r;
#pragma unroll
    for (int j = 0; j < 4; ++j) r[j] = bfround(gg * bfround(a[j])) + bfround(og * bfround(bb[j]));
    *(u32x2*)(out + idx) = cvt4(r);
}

extern "C" void kernel_launch(void* const* d_in, const int* in_sizes, int n_in, void* d_out, int out_size, void* d_ws,
                              size_t ws_size, hipStream_t stream) {
    (void)in_sizes; (void)n_in; (void)out_size; (void)ws_size;
    char* ws = (char*)d_ws;
    const size_t MB = 1u << 20;
    ProjArgs A;
    A.x = (const bf16_t*)d_in[0]; A.ckv = (const bf16_t*)d_in[1]; A.wq = (const bf16_t*)d_in[2];
    A.wuk = (const bf16_t*)d_in[3]; A.wuv = (const bf16_t*)d_in[4];
    const bf16_t* wo = (const bf16_t*)d_in[5];
    A.bwq = (const bf16_t*)d_in[7]; A.bwk = (const bf16_t*)d_in[8]; A.bwv = (const bf16_t*)d_in[9];
    const bf16_t* bwo = (const bf16_t*)d_in[10];
    A.gw = (const bf16_t*)d_in[11];
    A.Q = (bf16_t*)(ws + 0 * MB); A.K = (bf16_t*)(ws + 16 * MB); A.V = (bf16_t*)(ws + 32 * MB);
    A.qB = (bf16_t*)(ws + 48 * MB); A.kB = (bf16_t*)(ws + 50 * MB); A.vB = (bf16_t*)(ws + 51 * MB);
    A.g = (float*)(ws + 52 * MB);
    bf16_t* ctxA = (bf16_t*)(ws + 56 * MB);
    bf16_t* ctxB = (bf16_t*)(ws + 72 * MB);
    float* slabs = (float*)(ws + 80 * MB);

    proj_kernel<<<256, 512, 0, stream>>>(A);
    attnA_kernel<<<512, 512, 0, stream>>>(A.Q, A.K, A.V, ctxA);
    attnB_kernel<<<256, 512, 0, stream>>>(A.qB, A.kB, A.vB, ctxB);
    oproj_kernel<<<256, 256, 0, stream>>>(ctxA, wo, ctxB, bwo, slabs);
    reduce_kernel<<<1024, 256, 0, stream>>>(slabs, A.g, (bf16_t*)d_out);
}
```

```cpp
#include <hip/hip_runtime.h>
#include <hip/hip_bf16.h>
#include <stdint.h>

typedef unsigned short bf16_t;
typedef short bf16x8 __attribute__((ext_vector_type(8)));
typedef short s16x4 __attribute__((ext_vector_type(4)));
typedef float f32x16 __attribute__((ext_vector_type(16)));
typedef float f32x8 __attribute__((ext_vector_type(8)));
typedef float f32x4 __attribute__((ext_vector_type(4)));
typedef float f32x2 __attribute__((ext_vector_type(2)));
typedef unsigned u32x2 __attribute__((ext_vector_type(2)));
typedef unsigned u32x4 __attribute__((ext_vector_type(4)));
typedef __bf16 bf16v8 __attribute__((ext_vector_type(8)));
typedef __bf16 bf16v4 __attribute__((ext_vector_type(4)));
typedef __attribute__((address_space(3))) s16x4 lds_s16x4;

#define USE_SGB 1
#define MFMA32(a, b, c) __builtin_amdgcn_mfma_f32_32x32x16_bf16((a), (b), (c), 0, 0, 0)
#define VMCNT(n) asm volatile("s_waitcnt vmcnt(%0)" ::"n"(n) : "memory")
#define BARRIER() __builtin_amdgcn_s_barrier()

__device__ __forceinline__ bf16x8 cvt8(f32x8 v) { return __builtin_bit_cast(bf16x8, __builtin_convertvector(v, bf16v8)); }
__device__ __forceinline__ u32x2 cvt4(f32x4 v) { return __builtin_bit_cast(u32x2, __builtin_convertvector(v, bf16v4)); }
__device__ __forceinline__ float bf2f(short v) { return __uint_as_float(((unsigned)(unsigned short)v) << 16); }
__device__ __forceinline__ float bfround(float x) { return (float)(__bf16)x; }
__device__ __forceinline__ int crow(int r, int hh) { return (r & 3) + 8 * (r >> 2) + 4 * hh; }
__device__ __forceinline__ void glds16(const void* g, char* lds) {
    const unsigned a = __builtin_amdgcn_readfirstlane((unsigned)(size_t)((__attribute__((address_space(3))) char*)lds));
    unsigned keep;
    asm volatile("s_mov_b32 %0, m0\n\ts_mov_b32 m0, %2\n\ts_nop 0\n\tglobal_load_lds_dwordx4 %1, off\n\ts_mov_b32 m0, %0"
                 : "=&s"(keep) : "v"(g), "s"(a) : "memory");
}
__device__ __forceinline__ s16x4 trread(const char* p) { return __builtin_amdgcn_ds_read_tr16_b64_v4i16((lds_s16x4*)p); }
__device__ __forceinline__ bf16x8 cat4(s16x4 lo, s16x4 hi) { return __builtin_shufflevector(lo, hi, 0, 1, 2, 3, 4, 5, 6, 7); }

struct ProjArgs {
    const bf16_t* x; const bf16_t* ckv; const bf16_t* wq; const bf16_t* wuk; const bf16_t* wuv;
    const bf16_t* bwq; const bf16_t* bwk; const bf16_t* bwv; const bf16_t* gw;
    bf16_t* Q; bf16_t* K; bf16_t* V; bf16_t* qB; bf16_t* kB; bf16_t* vB; float* g;
};

__global__ __launch_bounds__(512, 1) void proj_kernel(ProjArgs A) {
    __shared__ __attribute__((aligned(1024))) char smem[4 * 32768 + 1024];
    const int tid = threadIdx.x, lane = tid & 63;
    const int w = __builtin_amdgcn_readfirstlane(tid >> 6);
    const int hh = lane >> 5, l31 = lane & 31;
    const int b = blockIdx.x;
    const int cg = (b & 7) * 4 + ((b >> 3) & 3), rb = b >> 5;
    const bool xtype = cg < 12;
    int t0, nt;
    if (xtype) { t0 = cg * 12; nt = (cg == 11) ? 13 : 12; }
    else { const int c = cg - 12; if (c < 16) { t0 = 13 * c; nt = 13; } else { t0 = 208 + 12 * (c - 16); nt = 12; } }
    const bf16_t* src = xtype ? A.x : A.ckv;
    const int row = rb * 256 + w * 32 + l31;

    auto issue = [&](int i) {
        const int tile = t0 + i;
        const bf16_t* W; int ldw, n0; bool special = false;
        if (xtype) {
            if (tile < 128) { W = A.wq; ldw = 4096; n0 = tile * 32; }
            else if (tile < 144) { W = A.bwq; ldw = 512; n0 = (tile - 128) * 32; }
            else { W = A.bwk; ldw = 16; n0 = 0; special = true; }
        } else {
            if (tile < 128) { W = A.wuk; ldw = 4096; n0 = tile * 32; }
            else { W = A.wuv; ldw = 4096; n0 = (tile - 128) * 32; }
        }
#pragma unroll
        for (int ii = 0; ii < 4; ++ii) {
            const int k = 16 * (4 * w + ii) + (lane >> 2), ch = lane & 3;
            const bf16_t* p = special ? ((ch < 2 ? A.bwk : A.bwv) + k * 16 + 8 * (ch & 1))
                                      : (W + (size_t)k * ldw + n0 + 8 * ch);
            glds16(p, smem + (i & 3) * 32768 + (4 * w + ii) * 1024);
        }
    };

    bf16x8 a[32];
    if (w == 0) glds16(A.gw + lane * 8, smem + 131072);
#pragma unroll
#define ABL_ROUNDS 2
    for (int r = 0; r < ABL_ROUNDS; ++r) {
#pragma unroll
        for (int j = 0; j < 16; ++j) {
            const int rl = 16 * w + j;
            glds16(src + (size_t)(rb * 256 + r * 128 + rl) * 512 + ((lane ^ (rl & 15)) * 8), smem + rl * 1024);
        }
        VMCNT(0); BARRIER();
        if ((w >> 2) == r) {
            const int rl = (w & 3) * 32 + l31;
#pragma unroll
            for (int s = 0; s < 32; ++s) a[s] = *(const bf16x8*)(smem + rl * 1024 + (((2 * s + hh) ^ (rl & 15)) << 4));
        }
        asm volatile("s_waitcnt lgkmcnt(0)" ::: "memory");
        BARRIER();
    }
    issue(0); issue(1); issue(2);

    if (cg == 0) {
        float sum = 0.f;
#pragma unroll
        for (int s = 0; s < 32; ++s) {
            const bf16x8 gw = *(const bf16x8*)(smem + 131072 + (2 * s + hh) * 16);
#pragma unroll
            for (int j = 0; j < 8; ++j) sum += bfround(bf2f(a[s][j]) * bf2f(gw[j]));
        }
        sum += __shfl_xor(sum, 32);
        if (hh == 0) {
            const float sb = bfround(sum);
            A.g[row] = bfround(1.0f / (1.0f + __expf(-sb)));
        }
    }

    const int g16 = lane >> 4, colhalf = g16 & 1, q4 = (lane & 15) >> 2, p4 = lane & 3;
    const int lane_off = (8 * hh + q4) * 64 + 32 * colhalf + 8 * p4;

    for (int i = 0; i < nt; ++i) {
        if (i == 0) VMCNT(8); else if (i == 1) VMCNT(12); else if (i == 2) VMCNT(16);
        else if (i + 2 < nt) VMCNT(20); else if (i + 1 < nt) VMCNT(16); else VMCNT(12);
        BARRIER();
        if (i + 3 < nt) issue(i + 3);
        const char* wb = smem + (i & 3) * 32768 + lane_off;
        f32x16 acc = {};
        constexpr int LA = 4;
        bf16x8 wf[LA];
#pragma unroll
        for (int s = 0; s < LA; ++s) wf[s] = cat4(trread(wb + s * 1024), trread(wb + s * 1024 + 256));
        __builtin_amdgcn_sched_group_barrier(0x100, 2 * LA, 0);
#pragma unroll
        for (int s = 0; s < 32; ++s) {
            acc = MFMA32(wf[s % LA], a[s], acc);
            if (s + LA < 32) wf[s % LA] = cat4(trread(wb + (s + LA) * 1024), trread(wb + (s + LA) * 1024 + 256));
            __builtin_amdgcn_sched_group_barrier(0x8, 1, 0);
            __builtin_amdgcn_sched_group_barrier(0x100, 2, 0);
        }
        const int tile = t0 + i;
        bf16_t* dst; bool special = false;
        if (xtype) {
            if (tile < 128) dst = A.Q + ((size_t)(tile >> 2) * 2048 + row) * 128 + (tile & 3) * 32;
            else if (tile < 144) dst = A.qB + (size_t)row * 512 + (tile - 128) * 32;
            else { dst = A.kB; special = true; }
        } else {
            bf16_t* base = tile < 128 ? A.K : A.V; const int t = tile & 127;
            dst = base + ((size_t)(t >> 2) * 2048 + row) * 128 + (t & 3) * 32;
        }
#pragma unroll
        for (int g4 = 0; g4 < 4; ++g4) {
            const u32x2 v = cvt4((f32x4){acc[4 * g4], acc[4 * g4 + 1], acc[4 * g4 + 2], acc[4 * g4 + 3]});
            bf16_t* d = special ? ((g4 < 2 ? A.kB : A.vB) + (size_t)row * 16 + 8 * (g4 & 1) + 4 * hh) : (dst + 8 * g4 + 4 * hh);
            *(u32x2*)d = v;
        }
    }
}

__global__ __launch_bounds__(512, 1) void attnA_kernel(const bf16_t* __restrict__ Qg, const bf16_t* __restrict__ Kg,
                                                       const bf16_t* __restrict__ Vg, bf16_t* __restrict__ ctx) {
    __shared__ __attribute__((aligned(1024))) char smem[163840];
    const int tid = threadIdx.x, lane = tid & 63;
    const int w = __builtin_amdgcn_readfirstlane(tid >> 6);
    const int rg = w & 3, kh = w >> 2, hh = lane >> 5, l31 = lane & 31;
    const int b = blockIdx.x, xq = 15 - (b >> 5), head = b & 31;
    const bf16_t* Qh = Qg + (size_t)head * 2048 * 128;
    const bf16_t* Kh = Kg + (size_t)head * 2048 * 128;
    const bf16_t* Vh = Vg + (size_t)head * 2048 * 128;
    const int qloc = rg * 32 + l31, qrow = xq * 128 + qloc;

    auto issue_k = [&](int t) {
        const int kb = t % 3;
#pragma unroll
        for (int j = 0; j < 4; ++j) {
            const int ii = 4 * w + j, r = 4 * ii + (lane >> 4), c = lane & 15;
            glds16(Kh + (size_t)(t * 128 + r) * 128 + ((c ^ (r & 15)) * 8), smem + kb * 32768 + ii * 1024);
        }
    };
    auto issue_v = [&](int t) {
#pragma unroll
        for (int j = 0; j < 4; ++j) {
            const int ii = 4 * w + j, r = 4 * ii + (lane >> 4), c = lane & 15;
            glds16(Vh + (size_t)(t * 128 + r) * 128 + ((c ^ ((r & 3) << 2)) * 8), smem + 98304 + (t & 1) * 32768 + ii * 1024);
        }
    };
    issue_k(0); issue_v(0); if (xq > 0) issue_k(1);

    bf16x8 qf[8];
#pragma unroll
    for (int d0 = 0; d0 < 8; ++d0) qf[d0] = *(const bf16x8*)(Qh + (size_t)qrow * 128 + 16 * d0 + 8 * hh);

    int kaddr[8], vaddr[4];
    {
        const int key = 64 * kh + l31, base = key * 256 + ((hh ^ (key & 15)) << 4);
#pragma unroll
        for (int d0 = 0; d0 < 8; ++d0) kaddr[d0] = base ^ (d0 << 5);
        const int dhalf = (lane >> 4) & 1, q4 = (lane & 15) >> 2, p4 = lane & 3;
#pragma unroll
        for (int db = 0; db < 4; ++db) vaddr[db] = 98304 + (64 * kh + 4 * hh + q4) * 256 + ((db ^ q4) * 64) + 32 * dhalf + 8 * p4;
    }
    const float csc = 0.08838834764831845f * 1.4426950408889634f;
    f32x16 o[4] = {};
    float m = -1e30f, l = 0.f;
    f32x16 s0, s1;

    auto qk = [&](int t, f32x16& a0, f32x16& a1) {
        const char* kb = smem + (t % 3) * 32768;
        a0 = f32x16{}; a1 = f32x16{};
#pragma unroll
        for (int d0 = 0; d0 < 8; ++d0) {
            const bf16x8 k0 = *(const bf16x8*)(kb + kaddr[d0]);
            const bf16x8 k1 = *(const bf16x8*)(kb + 8192 + kaddr[d0]);
            a0 = MFMA32(k0, qf[d0], a0);
            a1 = MFMA32(k1, qf[d0], a1);
        }
        if (t == xq) {
#pragma unroll
            for (int r = 0; r < 16; ++r) {
                if (64 * kh + crow(r, hh) > qloc) a0[r] = -__builtin_inff();
                if (64 * kh + 32 + crow(r, hh) > qloc) a1[r] = -__builtin_inff();
            }
        }
    };

    VMCNT(0); BARRIER();
    qk(0, s0, s1);
    for (int t = 0; t <= xq; ++t) {
        if (t + 2 <= xq) issue_k(t + 2);
        if (t + 1 <= xq) issue_v(t + 1);
        float smax = fmaxf(s0[0], s1[0]);
#pragma unroll
        for (int r = 1; r < 16; ++r) smax = fmaxf(smax, fmaxf(s0[r], s1[r]));
        if (__any((smax - m) * csc > 8.0f)) {
            const float rmax = fmaxf(smax, __shfl_xor(smax, 32));
            const float mnew = fmaxf(m, rmax);
            const float alpha = __builtin_amdgcn_exp2f((m - mnew) * csc);
            l *= alpha;
#pragma unroll
            for (int db = 0; db < 4; ++db)
#pragma unroll
                for (int r = 0; r < 16; ++r) o[db][r] *= alpha;
            m = mnew;
        }
        const float mc = m * csc;
        bf16x8 pf[4];
#pragma unroll
        for (int hb = 0; hb < 4; ++hb) {
            f32x8 p;
#pragma unroll
            for (int r = 0; r < 8; ++r) {
                const float sv = (hb < 2) ? s0[8 * hb + r] : s1[8 * (hb - 2) + r];
                p[r] = __builtin_amdgcn_exp2f(sv * csc - mc); l += p[r];
            }
            pf[hb] = cvt8(p);
        }
        f32x16 n0, n1;
        if (t < xq) qk(t + 1, n0, n1);
        const char* vb = smem + (t & 1) * 32768;
#pragma unroll
        for (int ks = 0; ks < 4; ++ks) {
#pragma unroll
            for (int db = 0; db < 4; ++db) {
                const char* vp = vb + vaddr[db] + ks * 4096;
                const bf16x8 vf = cat4(trread(vp), trread(vp + 2048));
                o[db] = MFMA32(vf, pf[ks], o[db]);
            }
        }
        if (t < xq) { s0 = n0; s1 = n1; }
        VMCNT(0); BARRIER();
    }
    l += __shfl_xor(l, 32);
    float* mo = (float*)smem;
    float* st = (float*)(smem + 65536);
    auto give = [&](const f32x16& a0, const f32x16& a1, int db0) {
#pragma unroll
        for (int r = 0; r < 16; ++r) {
            mo[(rg * 64 + db0 * 16 + r) * 64 + lane] = a0[r];
            mo[(rg * 64 + (db0 + 1) * 16 + r) * 64 + lane] = a1[r];
        }
    };
    if (kh == 0) give(o[2], o[3], 2); else give(o[0], o[1], 0);
    st[((rg * 2 + kh) * 2 + 0) * 64 + lane] = m;
    st[((rg * 2 + kh) * 2 + 1) * 64 + lane] = l;
    __syncthreads();
    {
        const float m1 = st[((rg * 2 + (kh ^ 1)) * 2 + 0) * 64 + lane], l1 = st[((rg * 2 + (kh ^ 1)) * 2 + 1) * 64 + lane];
        const float mm = fmaxf(m, m1);
        const float a0 = __builtin_amdgcn_exp2f((m - mm) * csc), a1 = __builtin_amdgcn_exp2f((m1 - mm) * csc);
        const float inv = 1.0f / (l * a0 + l1 * a1);
        bf16_t* dst = ctx + (size_t)qrow * 4096 + head * 128 + 4 * hh;
        auto fin = [&](const f32x16& oo, int db) {
#pragma unroll
            for (int g4 = 0; g4 < 4; ++g4) {
                f32x4 v;
#pragma unroll
                for (int j = 0; j < 4; ++j) {
                    const int r = 4 * g4 + j;
                    v[j] = (oo[r] * a0 + mo[(rg * 64 + db * 16 + r) * 64 + lane] * a1) * inv;
                }
                *(u32x2*)(dst + db * 32 + 8 * g4) = cvt4(v);
            }
        };
        if (kh == 0) { fin(o[0], 0); fin(o[1], 1); } else { fin(o[2], 2); fin(o[3], 3); }
    }
}

__global__ __launch_bounds__(512, 1) void attnB_kernel(const bf16_t* __restrict__ qB, const bf16_t* __restrict__ kB,
                                                       const bf16_t* __restrict__ vB, bf16_t* __restrict__ ctxB) {
    __shared__ __attribute__((aligned(1024))) char smem[131072 + 1024];
    const int tid = threadIdx.x, lane = tid & 63;
    const int w = __builtin_amdgcn_readfirstlane(tid >> 6);
    const int hh = lane >> 5, l31 = lane & 31;
#pragma unroll
    for (int j = 0; j < 8; ++j) {
        const int ci = (8 * w + j) * 64 + lane;
        glds16(kB + (size_t)(ci & 2047) * 16 + 8 * (ci >> 11), smem + (8 * w + j) * 1024);
        glds16(vB + (size_t)ci * 8, smem + 65536 + (8 * w + j) * 1024);
    }
    {
        const int c = tid & 15;
        ((bf16_t*)(smem + 131072))[tid] = (c == 0 || c == 4) ? (bf16_t)0x3F80 : (bf16_t)0;
    }
    const int qp = blockIdx.x * 8 + w;
    const bf16x8 qf = *(const bf16x8*)(qB + (size_t)qp * 512 + l31 * 16 + 8 * hh);
    VMCNT(0);
    __syncthreads();

    const int dhalf = (lane >> 4) & 1, q4 = (lane & 15) >> 2, p4 = lane & 3;
    const int vbase = dhalf ? (131072 + 8 * p4) : (65536 + (4 * hh + q4) * 32 + 8 * p4);
    const int vstep = dhalf ? 0 : 1024;
    const int kbase = (hh * 2048 + l31) * 16;
    const float csc = 0.25f * 1.4426950408889634f;
    f32x16 o = {};
    float m = -1e30f;
    for (int kt = 0; kt < 64; kt += 4) {
        f32x16 s[4];
#pragma unroll
        for (int i = 0; i < 4; ++i) {
            const bf16x8 kf = *(const bf16x8*)(smem + kbase + (kt + i) * 512);
            f32x16 z = {};
            s[i] = MFMA32(kf, qf, z);
        }
        float smax = s[0][0];
#pragma unroll
        for (int i = 0; i < 4; ++i)
#pragma unroll
            for (int r = 0; r < 16; ++r) smax = fmaxf(smax, s[i][r]);
        if (__any((smax - m) * csc > 8.0f)) {
            const float rmax = fmaxf(smax, __shfl_xor(smax, 32));
            const float mnew = fmaxf(m, rmax);
            const float alpha = __builtin_amdgcn_exp2f((m - mnew) * csc);
#pragma unroll
            for (int r = 0; r < 16; ++r) o[r] *= alpha;
            m = mnew;
        }
        const float mc = m * csc;
#pragma unroll
        for (int i = 0; i < 4; ++i) {
            f32x8 p0, p1;
#pragma unroll
            for (int r = 0; r < 8; r += 2) {
                const f32x2 t = __builtin_elementwise_fma((f32x2){s[i][r], s[i][r + 1]}, (f32x2){csc, csc}, (f32x2){-mc, -mc});
                p0[r] = __builtin_amdgcn_exp2f(t[0]); p0[r + 1] = __builtin_amdgcn_exp2f(t[1]);
            }
#pragma unroll
            for (int r = 0; r < 8; r += 2) {
                const f32x2 t = __builtin_elementwise_fma((f32x2){s[i][8 + r], s[i][9 + r]}, (f32x2){csc, csc}, (f32x2){-mc, -mc});
                p1[r] = __builtin_amdgcn_exp2f(t[0]); p1[r + 1] = __builtin_amdgcn_exp2f(t[1]);
            }
            const bf16x8 pf0 = cvt8(p0), pf1 = cvt8(p1);
            const char* vp = smem + vbase + (kt + i) * vstep;
            const bf16x8 vf0 = cat4(trread(vp), trread(vp + 256));
            const bf16x8 vf1 = cat4(trread(vp + 512), trread(vp + 768));
            o = MFMA32(vf0, pf0, o);
            o = MFMA32(vf1, pf1, o);
        }
    }
    const float inv = 1.0f / o[8];
    bf16_t* dst = ctxB + (size_t)qp * 512 + l31 * 16 + 4 * hh;
    *(u32x2*)(dst) = cvt4((f32x4){o[0] * inv, o[1] * inv, o[2] * inv, o[3] * inv});
    *(u32x2*)(dst + 8) = cvt4((f32x4){o[4] * inv, o[5] * inv, o[6] * inv, o[7] * inv});
}

__device__ __forceinline__ void gemm_job(const bf16_t* __restrict__ Ag, int lda, const bf16_t* __restrict__ Wg, int ldw,
                                         int k0, int klen, int m0, int n0, float* __restrict__ slab, char* smem,
                                         int lane, int w) {
    const int hh = lane >> 5, l31 = lane & 31;
    const int wm = w >> 1, wn = w & 1;
    const int nk = klen / 32;
    auto issue = [&](int kt) {
        const int kk = k0 + kt * 32;
        char* sb = smem + (kt & 3) * 16384;
#pragma unroll
        for (int j = 0; j < 2; ++j) {
            const int ii = 2 * w + j;
            {
                const int r = 16 * ii + (lane >> 2), c = lane & 3;
                glds16(Ag + (size_t)(m0 + r) * lda + kk + ((c ^ ((r >> 2) & 3)) * 8), sb + ii * 1024);
            }
            {
                const int sub = ii >> 1, k = 16 * (ii & 1) + (lane >> 2), ch = lane & 3;
                glds16(Wg + (size_t)(kk + k) * ldw + n0 + sub * 32 + 8 * ch, sb + 8192 + ii * 1024);
            }
        }
    };
    f32x16 acc[2][2] = {};
    const int g16 = lane >> 4, colhalf = g16 & 1, q4 = (lane & 15) >> 2, p4 = lane & 3;
    const int woff = (8 * hh + q4) * 64 + 32 * colhalf + 8 * p4;
    issue(0); issue(1); issue(2);
    for (int kt = 0; kt < nk; ++kt) {
        if (kt + 2 < nk) VMCNT(8); else if (kt + 1 < nk) VMCNT(4); else VMCNT(0);
        BARRIER();
        if (kt + 3 < nk) issue(kt + 3);
        const char* ab = smem + (kt & 3) * 16384;
        const char* wb = ab + 8192 + woff;
#pragma unroll
        for (int s = 0; s < 2; ++s) {
            bf16x8 af[2], bfr[2];
#pragma unroll
            for (int mi = 0; mi < 2; ++mi) {
                const int r = wm * 64 + mi * 32 + l31;
                af[mi] = *(const bf16x8*)(ab + r * 64 + (((2 * s + hh) ^ ((r >> 2) & 3)) << 4));
            }
#pragma unroll
            for (int ni = 0; ni < 2; ++ni) {
                const char* p = wb + (wn * 2 + ni) * 2048 + s * 1024;
                bfr[ni] = cat4(trread(p), trread(p + 256));
            }
#pragma unroll
            for (int mi = 0; mi < 2; ++mi)
#pragma unroll
                for (int ni = 0; ni < 2; ++ni) acc[mi][ni] = MFMA32(af[mi], bfr[ni], acc[mi][ni]);
        }
    }
#pragma unroll
    for (int mi = 0; mi < 2; ++mi)
#pragma unroll
        for (int ni = 0; ni < 2; ++ni)
#pragma unroll
            for (int r = 0; r < 16; ++r) {
                const int mrow = m0 + wm * 64 + mi * 32 + crow(r, hh), ncol = n0 + wn * 64 + ni * 32 + l31;
                slab[(size_t)mrow * 512 + ncol] = acc[mi][ni][r];
            }
    __syncthreads();
}

__global__ __launch_bounds__(256, 2) void oproj_kernel(const bf16_t* __restrict__ ctxA, const bf16_t* __restrict__ wo,
                                                       const bf16_t* __restrict__ ctxB, const bf16_t* __restrict__ bwo,
                                                       float* __restrict__ slabs) {
    __shared__ __attribute__((aligned(1024))) char smem[65536];
    const int tid = threadIdx.x, lane = tid & 63;
    const int w = __builtin_amdgcn_readfirstlane(tid >> 6);
    const int b = blockIdx.x;
    const int z = b & 3, nt = (b >> 2) & 3, mt = b >> 4;
    const int m0 = mt * 128, n0 = nt * 128;
    const size_t SL = (size_t)2048 * 512;
    if (z < 3) {
        gemm_job(ctxA, 4096, wo, 512, 1152 * z, 1152, m0, n0, slabs + z * SL, smem, lane, w);
    } else {
        gemm_job(ctxA, 4096, wo, 512, 3456, 640, m0, n0, slabs + 3 * SL, smem, lane, w);
        gemm_job(ctxB, 512, bwo, 512, 0, 512, m0, n0, slabs + 4 * SL, smem, lane, w);
    }
}

__global__ __launch_bounds__(256) void reduce_kernel(const float* __restrict__ slabs, const float* __restrict__ g,
                                                     bf16_t* __restrict__ out) {
    const size_t SL = (size_t)2048 * 512;
    const int idx = (blockIdx.x * 256 + threadIdx.x) * 4;
    const int row = idx >> 9;
    f32x4 a = *(const f32x4*)(slabs + idx);
#pragma unroll
    for (int z = 1; z < 4; ++z) a += *(const f32x4*)(slabs + z * SL + idx);
    const f32x4 bb = *(const f32x4*)(slabs + 4 * SL + idx);
    const float gg = g[row], og = bfround(1.0f - gg);
    f32x4 r;
#pragma unroll
    for (int j = 0; j < 4; ++j) r[j] = bfround(gg * bfround(a[j])) + bfround(og * bfround(bb[j]));
    *(u32x2*)(out + idx) = cvt4(r);
}

extern "C" void kernel_launch(void* const* d_in, const int* in_sizes, int n_in, void* d_out, int out_size, void* d_ws,
                              size_t ws_size, hipStream_t stream) {
    (void)in_sizes; (void)n_in; (void)out_size; (void)ws_size;
    char* ws = (char*)d_ws;
    const size_t MB = 1u << 20;
    ProjArgs A;
    A.x = (const bf16_t*)d_in[0]; A.ckv = (const bf16_t*)d_in[1]; A.wq = (const bf16_t*)d_in[2];
    A.wuk = (const bf16_t*)d_in[3]; A.wuv = (const bf16_t*)d_in[4];
    const bf16_t* wo = (const bf16_t*)d_in[5];
    A.bwq = (const bf16_t*)d_in[7]; A.bwk = (const bf16_t*)d_in[8]; A.bwv = (const bf16_t*)d_in[9];
    const bf16_t* bwo = (const bf16_t*)d_in[10];
    A.gw = (const bf16_t*)d_in[11];
    A.Q = (bf16_t*)(ws + 0 * MB); A.K = (bf16_t*)(ws + 16 * MB); A.V = (bf16_t*)(ws + 32 * MB);
    A.qB = (bf16_t*)(ws + 48 * MB); A.kB = (bf16_t*)(ws + 50 * MB); A.vB = (bf16_t*)(ws + 51 * MB);
    A.g = (float*)(ws + 52 * MB);
    bf16_t* ctxA = (bf16_t*)(ws + 56 * MB);
    bf16_t* ctxB = (bf16_t*)(ws + 72 * MB);
    float* slabs = (float*)(ws + 80 * MB);

    proj_kernel<<<256, 512, 0, stream>>>(A);
    attnA_kernel<<<512, 512, 0, stream>>>(A.Q, A.K, A.V, ctxA);
    attnB_kernel<<<256, 512, 0, stream>>>(A.qB, A.kB, A.vB, ctxB);
    oproj_kernel<<<256, 256, 0, stream>>>(ctxA, wo, ctxB, bwo, slabs);
    reduce_kernel<<<1024, 256, 0, stream>>>(slabs, A.g, (bf16_t*)d_out);
}
```
